```python
import torch

def _softmax(x, axis=-1):
    return torch.softmax(x, dim=axis)
def _gelu(x):
    return torch.nn.functional.gelu(x, approximate="tanh")
def _silu(x):
    return torch.nn.functional.silu(x)
def _sigmoid(x):
    return torch.sigmoid(x)
def _layernorm(x, axis=-1, eps=1e-5):
    m = x.mean(dim=axis, keepdim=True)
    v = x.var(dim=axis, keepdim=True, unbiased=False)
    return (x - m) / torch.sqrt(v + eps)
def _scatter_add(tgt, idx, src, axis=0):
    return tgt.clone().index_add_(axis, idx, src)
def _segment_sum(src, seg, n):
    out = torch.zeros((n,) + src.shape[1:], dtype=src.dtype)
    return out.index_add_(0, seg, src)
def _pad(x, pw):
    flat = tuple(v for pair in reversed(pw) for v in pair)
    return torch.nn.functional.pad(x, flat)

kernel_name = 'standalone__gated_linear_recurrence__prompt'

def setup_inputs():

    _w = (lambda a: (lambda t: t.to(torch.bfloat16) if t.is_floating_point() else t)(torch.as_tensor(a)))
    B, H, T, D = 1, 32, 2048, 128
    return {
        "q": _w((lambda *s: torch.randn(*s, dtype=torch.bfloat16) * 0.05)(B, H, T, D)),
        "k": _w((lambda *s: torch.randn(*s, dtype=torch.bfloat16) * 0.05)(B, H, T, D)),
        "v": _w((lambda *s: torch.randn(*s, dtype=torch.bfloat16) * 0.05)(B, H, T, D)),
        "g": _w((lambda *s: torch.randn(*s, dtype=torch.bfloat16) * 0.8)(B, H, T, D)),
    }

def reference(q, k, v, g):
    B, H, T, D = q.shape
    scale = D ** -0.5
    a = _sigmoid(g)
    S = k[:, :, 0, :, None] * v[:, :, 0, None, :] * 0.0
    outs = []
    for t in range(T):
        S = a[:, :, t, :, None] * S + k[:, :, t, :, None] * v[:, :, t, None, :]
        ot = torch.einsum("bhd,bhde->bhe", q[:, :, t, :], S) * scale
        outs.append(ot)
    return torch.stack(outs, 2)
```

```cpp
#include <hip/hip_runtime.h>
#include <hip/hip_bf16.h>
typedef unsigned short u16;
__device__ inline float bf2f(u16 x){ return __uint_as_float(((unsigned)x)<<16); }
__global__ __launch_bounds__(128) void simple_k(const u16* q,const u16* k,const u16* v,const u16* g, __hip_bfloat16* out){
  const int h = blockIdx.x>>3, es = blockIdx.x&7; const int d = threadIdx.x;
  __shared__ float red[2][16];
  float S[16];
  #pragma unroll
  for(int e=0;e<16;e++) S[e]=0.f;
  const size_t base=(size_t)h*2048*128;
  for(int t=0;t<2048;t++){
    size_t o=base+(size_t)t*128;
    float a=1.f/(1.f+__expf(-bf2f(g[o+d]))); float kk=bf2f(k[o+d]); float qq=bf2f(q[o+d]);
    float p[16];
    #pragma unroll
    for(int e=0;e<16;e++){ float vv=bf2f(v[o+es*16+e]); S[e]=a*S[e]+kk*vv; p[e]=qq*S[e]; }
    #pragma unroll
    for(int e=0;e<16;e++){
      #pragma unroll
      for(int m=32;m>=1;m>>=1) p[e]+=__shfl_xor(p[e],m);
    }
    if((d&63)==0){
      #pragma unroll
      for(int e=0;e<16;e++) red[d>>6][e]=p[e];
    }
    __syncthreads();
    if(d<16) out[o+es*16+d]=__float2bfloat16((red[0][d]+red[1][d])*0.08838834764831845f);
    __syncthreads();
  }
}
extern "C" void kernel_launch(void* const* d_in, const int* in_sizes, int n_in, void* d_out, int out_size, void* d_ws, size_t ws_size, hipStream_t stream) {
  simple_k<<<256,128,0,stream>>>((const u16*)d_in[0],(const u16*)d_in[1],(const u16*)d_in[2],(const u16*)d_in[3],(__hip_bfloat16*)d_out);
}
```

```cpp
#include <hip/hip_runtime.h>
#include <hip/hip_bf16.h>

typedef unsigned u32;
typedef unsigned long long u64;
typedef u32 u32x4 __attribute__((ext_vector_type(4)));
typedef u32 u32x2 __attribute__((ext_vector_type(2)));
typedef short s16x4 __attribute__((ext_vector_type(4)));
typedef __bf16 bf16x8 __attribute__((ext_vector_type(8)));
typedef __bf16 bf16x2 __attribute__((ext_vector_type(2)));
typedef float f32x2 __attribute__((ext_vector_type(2)));
typedef float f32x4 __attribute__((ext_vector_type(4)));
typedef float f32x16 __attribute__((ext_vector_type(16)));
#define LAS __attribute__((address_space(3)))
#define GAS __attribute__((address_space(1)))
#define DI __device__ __forceinline__
#define RLX_AGENT __ATOMIC_RELAXED, __HIP_MEMORY_SCOPE_AGENT

constexpr int kT = 2048, kSEG = 256, kNSEG = 8;
constexpr int LDS_K = 0, LDS_V = 65536, LDS_TOT = 131072, LDS_TAB = LDS_TOT + 4096;
constexpr int LDS_DW = LDS_TAB + 9 * 512;
constexpr int LDS_FLAG = LDS_DW + 8 * 1024;
constexpr int LDS_BYTES = LDS_FLAG + 16;
constexpr int WS_ROWS_BYTES = 1  __H0__ ;
constexpr int PUB_WAVE = 4096 + 128;
constexpr int PUB_SEG = 8 * PUB_WAVE;
constexpr int PUB_HEAD = 8 * PUB_SEG;
constexpr int WS_STAMP = WS_ROWS_BYTES + 32 * PUB_HEAD;

DI float bflo(u32 x) { return __uint_as_float(x << 16); }
DI float bfhi(u32 x) { return __uint_as_float(x & 0xffff0000u); }
DI u32 pk(float a, float b) { f32x2 f = {a, b}; return __builtin_bit_cast(u32, __builtin_convertvector(f, bf16x2)); }
DI f32x4 mfma16(u32x4 a, u32x4 b, f32x4 c) { return __builtin_amdgcn_mfma_f32_16x16x32_bf16(__builtin_bit_cast(bf16x8, a), __builtin_bit_cast(bf16x8, b), c, 0, 0, 0); }
DI f32x16 mfma32(u32x4 a, u32x4 b, f32x16 c) { return __builtin_amdgcn_mfma_f32_32x32x16_bf16(__builtin_bit_cast(bf16x8, a), __builtin_bit_cast(bf16x8, b), c, 0, 0, 0); }
DI u32x2 trrd(u32 addr) { return __builtin_bit_cast(u32x2, __builtin_amdgcn_ds_read_tr16_b64_v4i16((LAS s16x4*)(size_t)addr)); }
DI u32x4 trfrag(u32 a0, u32 a1) { u32x2 x = trrd(a0), y = trrd(a1); return (u32x4){x[0], x[1], y[0], y[1]}; }
DI u32x4 ldsr128(u32 addr) { return *(LAS u32x4*)(size_t)addr; }
DI f32x4 ldsr128f(u32 addr) { return *(LAS f32x4*)(size_t)addr; }
DI void ldsw128(u32 addr, u32x4 v) { *(LAS u32x4*)(size_t)addr = v; }
DI void barrier() { asm volatile("s_waitcnt lgkmcnt(0)\n\ts_barrier" ::: "memory"); }
#define PIN(x) asm volatile("" : "+v"(x))
DI u64 mix64(u64 z) { z += 0x9E3779B97F4A7C15ull; z = (z ^ (z >> 30)) * 0xBF58476D1CE4E5B9ull; z = (z ^ (z >> 27)) * 0x94D049BB133111EBull; return z ^ (z >> 31); }

#define STAMP(n) do {} while (0)

#define DPP8(CTRL) \
  "v_mul_f32_dpp %0, %0, %0 " CTRL " row_mask:0xf bank_mask:0xf\n\t" \
  "v_mul_f32_dpp %1, %1, %1 " CTRL " row_mask:0xf bank_mask:0xf\n\t" \
  "v_mul_f32_dpp %2, %2, %2 " CTRL " row_mask:0xf bank_mask:0xf\n\t" \
  "v_mul_f32_dpp %3, %3, %3 " CTRL " row_mask:0xf bank_mask:0xf\n\t" \
  "v_mul_f32_dpp %4, %4, %4 " CTRL " row_mask:0xf bank_mask:0xf\n\t" \
  "v_mul_f32_dpp %5, %5, %5 " CTRL " row_mask:0xf bank_mask:0xf\n\t" \
  "v_mul_f32_dpp %6, %6, %6 " CTRL " row_mask:0xf bank_mask:0xf\n\t" \
  "v_mul_f32_dpp %7, %7, %7 " CTRL " row_mask:0xf bank_mask:0xf\n\t"
#define DPP8X(CTRL) \
  "v_mul_f32_dpp %8, %0, %8 " CTRL " row_mask:0xf bank_mask:0xf\n\t" \
  "v_mul_f32_dpp %9, %1, %9 " CTRL " row_mask:0xf bank_mask:0xf\n\t" \
  "v_mul_f32_dpp %10, %2, %10 " CTRL " row_mask:0xf bank_mask:0xf\n\t" \
  "v_mul_f32_dpp %11, %3, %11 " CTRL " row_mask:0xf bank_mask:0xf\n\t" \
  "v_mul_f32_dpp %12, %4, %12 " CTRL " row_mask:0xf bank_mask:0xf\n\t" \
  "v_mul_f32_dpp %13, %5, %13 " CTRL " row_mask:0xf bank_mask:0xf\n\t" \
  "v_mul_f32_dpp %14, %6, %14 " CTRL " row_mask:0xf bank_mask:0xf\n\t" \
  "v_mul_f32_dpp %15, %7, %15 " CTRL " row_mask:0xf bank_mask:0xf\n\t"
#define OPS16(x, y) "+v"(x[0]), "+v"(x[1]), "+v"(x[2]), "+v"(x[3]), "+v"(x[4]), "+v"(x[5]), "+v"(x[6]), "+v"(x[7]), \
                    "+v"(y[0]), "+v"(y[1]), "+v"(y[2]), "+v"(y[3]), "+v"(y[4]), "+v"(y[5]), "+v"(y[6]), "+v"(y[7])
DI void scan_up(float (&x)[8], float (&y)[8]) {
  asm("s_nop 1\n\t" DPP8("row_shr:1") DPP8("row_shr:2") DPP8("row_shr:4") DPP8("row_shr:8") DPP8X("row_shr:1") : OPS16(x, y));
}
DI void scan_dn(float (&x)[8], float (&y)[8]) {
  asm("s_nop 1\n\t" DPP8("row_shl:1") DPP8("row_shl:2") DPP8("row_shl:4") DPP8("row_shl:8") DPP8X("row_shl:1") : OPS16(x, y));
}
DI float rinv(float g) { return 1.0f + __builtin_amdgcn_exp2f(g * -1.4426950408889634f); }

template <int UP>
DI void gates(const u32x4 (&graw)[2][4], float (&F)[2][4][8], u32 tot_addr, int i) {
#pragma unroll
  for (int ks = 0; ks < 4; ++ks) {
    float r0[8], r1[8], P[8];
#pragma unroll
    for (int j = 0; j < 4; ++j) {
      r0[2 * j] = rinv(bflo(graw[0][ks][j])); r0[2 * j + 1] = rinv(bfhi(graw[0][ks][j]));
      r1[2 * j] = rinv(bflo(graw[1][ks][j])); r1[2 * j + 1] = rinv(bfhi(graw[1][ks][j]));
    }
#pragma unroll
    for (int j = 0; j < 8; ++j) P[j] = r0[j] * r1[j];
    if (UP) {
      scan_up(P, r0);
#pragma unroll
      for (int j = 0; j < 8; ++j) { F[0][ks][j] = r0[j]; F[1][ks][j] = P[j]; }
    } else {
      float X[8];
#pragma unroll
      for (int j = 0; j < 8; ++j) X[j] = 1.0f;
      scan_dn(P, X);
#pragma unroll
      for (int j = 0; j < 8; ++j) { F[0][ks][j] = X[j] * r1[j]; F[1][ks][j] = X[j]; }
    }
    if (i == (UP ? 15 : 0)) {
      *(LAS f32x4*)(size_t)(tot_addr + ks * 128) = (f32x4){P[0], P[1], P[2], P[3]};
      *(LAS f32x4*)(size_t)(tot_addr + ks * 128 + 16) = (f32x4){P[4], P[5], P[6], P[7]};
    }
  }
}

template <int UP>
DI void make_k(const u32x4 (&kraw)[2][4], float (&F)[2][4][8], u32 a0, u32 a1) {
  const float scale = 0.08838834764831845f;
#pragma unroll
  for (int rho = 0; rho < 2; ++rho)
#pragma unroll
    for (int ks = 0; ks < 4; ++ks) {
      u32x4 o;
#pragma unroll
      for (int j = 0; j < 4; ++j) {
        float f0 = F[rho][ks][2 * j], f1 = F[rho][ks][2 * j + 1];
        float i0 = __builtin_amdgcn_rcpf(f0), i1 = __builtin_amdgcn_rcpf(f1);
        o[j] = pk(bflo(kraw[rho][ks][j]) * (UP ? f0 : i0), bfhi(kraw[rho][ks][j]) * (UP ? f1 : i1));
        F[rho][ks][2 * j] = (UP ? i0 : f0) * scale; F[rho][ks][2 * j + 1] = (UP ? i1 : f1) * scale;
        PIN(F[rho][ks][2 * j]); PIN(F[rho][ks][2 * j + 1]);
      }
      ldsw128((rho ? a1 : a0) + ks * 512, o);
    }
}

__global__ __launch_bounds__(512, 2) void gla_kernel(const void* __restrict__ qp, const void* __restrict__ kp, const void* __restrict__ vp,
                                                     const void* __restrict__ gp, void* __restrict__ op, char* ws) {
  extern __shared__ __attribute__((aligned(16))) char smem[];
  const u32 sb = (u32)(size_t)(LAS char*)smem;
  const int tid = threadIdx.x, lane = tid & 63, w = __builtin_amdgcn_readfirstlane(tid >> 6);
  const int bid = blockIdx.x, head = (bid & 7) * 4 + (bid >> 6), seg = (bid >> 3) & 7;
  const int c = w & 3, hf = w >> 2, tb = 64 * c + 32 * hf;
  const int i = lane & 15, qg = lane >> 4;
  const size_t gbase = ((size_t)head * kT + (size_t)seg * kSEG) * 256;
  const __amdgpu_buffer_rsrc_t rq = __builtin_amdgcn_make_buffer_rsrc((char*)qp + gbase, 0, 65536, 0x00020000);
  const __amdgpu_buffer_rsrc_t rk = __builtin_amdgcn_make_buffer_rsrc((char*)kp + gbase, 0, 65536, 0x00020000);
  const __amdgpu_buffer_rsrc_t rv = __builtin_amdgcn_make_buffer_rsrc((char*)vp + gbase, 0, 65536, 0x00020000);
  const __amdgpu_buffer_rsrc_t rg = __builtin_amdgcn_make_buffer_rsrc((char*)gp + gbase, 0, 65536, 0x00020000);
  const __amdgpu_buffer_rsrc_t ro = __builtin_amdgcn_make_buffer_rsrc((char*)op + gbase, 0, 65536, 0x00020000);
  STAMP(0);
  GAS u64* row = (GAS u64*)(ws + (head * 8 + w) * 128);
  const u64 epoch = __hip_atomic_load(row + 7, RLX_AGENT);

  const u32 foff = (u32)((tb + 2 * i) * 256 + qg * 16);
  u32x4 graw[2][4], kraw[2][4], qraw[2][4], vraw[8];
#pragma unroll
  for (int ks = 0; ks < 4; ++ks)
#pragma unroll
    for (int rho = 0; rho < 2; ++rho) graw[rho][ks] = __builtin_amdgcn_raw_buffer_load_b128(rg, foff + rho * 256 + ks * 64, 0, 0);
#pragma unroll
  for (int rho = 0; rho < 2; ++rho)
#pragma unroll
    for (int ks = 0; ks < 4; ++ks) kraw[rho][ks] = __builtin_amdgcn_raw_buffer_load_b128(rk, foff + rho * 256 + ks * 64, 0, 0);
  const u32 voff = (u32)((4 * w + qg) * 256 + i * 16);
#pragma unroll
  for (int n = 0; n < 8; ++n) vraw[n] = __builtin_amdgcn_raw_buffer_load_b128(rv, voff, n * 8192, 0);
#pragma unroll
  for (int ks = 0; ks < 4; ++ks) qraw[0][ks] = __builtin_amdgcn_raw_buffer_load_b128(rq, foff + ks * 64, 0, 0);
  __builtin_amdgcn_sched_barrier(0);
  STAMP(1);

  float F[2][4][8];
  const u32 tot_addr = sb + LDS_TOT + w * 512 + qg * 32;
  const int kr0 = tb + 2 * i;
  const u32 kw0 = sb + LDS_K + 2048 * (kr0 >> 3) + 64 * (kr0 & 7) + 16 * (qg ^ ((kr0 >> 2) & 3));
  if (hf) { gates<1>(graw, F, tot_addr, i); STAMP(2); make_k<1>(kraw, F, kw0, kw0 + 64); }
  else    { gates<0>(graw, F, tot_addr, i); STAMP(2); make_k<0>(kraw, F, kw0, kw0 + 64); }
  if (tid == 0) *(LAS u32*)(size_t)(sb + LDS_FLAG) = 0;
  barrier();
  STAMP(3);
  if (w < 2) {
    float f[8]; bool bad = false;
#pragma unroll
    for (int x = 0; x < 8; ++x) { float t = *(LAS float*)(size_t)(sb + LDS_TOT + x * 512 + tid * 4); bad |= !(t <= 0x1p62f); f[x] = __builtin_amdgcn_rcpf(t); }
    LAS float* tab = (LAS float*)(size_t)(sb + LDS_TAB) + tid;
    float psi0 = f[4] * f[1], psi1 = f[5] * f[2], psi2 = f[6] * f[3];
    tab[0] = psi0; tab[128] = psi1; tab[256] = psi2; tab[384] = f[7];
    float p0 = f[0], p1 = p0 * psi0, p2 = p1 * psi1, p3 = p2 * psi2;
    tab[512] = p0; tab[640] = p1; tab[768] = p2; tab[896] = p3; tab[1024] = p3 * f[7];
    if (bad) *(LAS u32*)(size_t)(sb + LDS_FLAG) = 1;
  }

  const int db = w >> 1, eh = w & 1, h = lane >> 5, blk = (lane >> 4) & 1, a4 = (lane & 15) >> 2, p = lane & 3;
  const int vr = 4 * w + qg;
  const u32 vw = sb + LDS_V + 2048 * (vr >> 3) + 512 * (i >> 2) + 64 * (vr & 7) + 16 * ((i & 3) ^ ((vr >> 2) & 3));
  u32 ka[2], va[2];
#pragma unroll
  for (int t = 0; t < 2; ++t) {
    const int x = (2 * h + t) & 3;
    ka[t] = sb + LDS_K + 2048 * h + 512 * db + 64 * (4 * t + a4) + 16 * ((2 * blk + (p & 1)) ^ x) + 8 * (p >> 1);
    va[t] = sb + LDS_V + 2048 * h + 1024 * eh + 64 * (4 * t + a4) + 16 * ((2 * blk + (p >> 1)) ^ x) + 8 * (p & 1);
  }
  const u32 tabr = sb + LDS_TAB + (32 * db + 8 * h) * 4;
  f32x16 acc[2];
#pragma unroll
  for (int r = 0; r < 16; ++r) { acc[0][r] = 0.f; acc[1][r] = 0.f; }
  u32x4 sv[4][2][2];
#pragma unroll
  for (int cc = 0; cc < 4; ++cc) {
    ldsw128(vw + (2 * cc) * 8192, vraw[2 * cc]);
    ldsw128(vw + (2 * cc + 1) * 8192, vraw[2 * cc + 1]);
    barrier();
#pragma unroll
    for (int kk = 4 * cc; kk < 4 * cc + 4; ++kk) {
      u32x4 af = trfrag(ka[0] + kk * 4096, ka[1] + kk * 4096);
      u32x4 b0 = trfrag(va[0] + kk * 4096, va[1] + kk * 4096);
      u32x4 b1 = trfrag(va[0] + kk * 4096 + 512, va[1] + kk * 4096 + 512);
      acc[0] = mfma32(af, b0, acc[0]);
      acc[1] = mfma32(af, b1, acc[1]);
    }
#pragma unroll
    for (int s = 0; s < 2; ++s) {
      f32x4 f0 = ldsr128f(tabr + cc * 512 + s * 64), f1 = ldsr128f(tabr + cc * 512 + s * 64 + 16);
#pragma unroll
      for (int e2 = 0; e2 < 2; ++e2) {
#pragma unroll
        for (int j = 0; j < 4; ++j) { acc[e2][8 * s + j] *= f0[j]; acc[e2][8 * s + 4 + j] *= f1[j]; }
#pragma unroll
        for (int j = 0; j < 4; ++j) sv[cc][e2][s][j] = pk(acc[e2][8 * s + 2 * j], acc[e2][8 * s + 2 * j + 1]);
        PIN(sv[cc][e2][s]);
      }
    }
  }
  STAMP(4);
  char* pubh = ws + WS_ROWS_BYTES + (size_t)head * PUB_HEAD + (7 - seg) * PUB_SEG + w * PUB_WAVE;
  {
    const __amdgpu_buffer_rsrc_t rp = __builtin_amdgcn_make_buffer_rsrc(pubh, 0, seg < 7 ? PUB_WAVE : 0, 0x00020000);
#pragma unroll
    for (int e2 = 0; e2 < 2; ++e2)
#pragma unroll
      for (int s = 0; s < 2; ++s) __builtin_amdgcn_raw_buffer_store_b128(sv[3][e2][s], rp, lane * 16 + (e2 * 2 + s) * 1024, 0, 16);
    u32x4 dsl = ldsr128(sb + LDS_TAB + 8 * 512 + db * 128 + (lane & 7) * 16);
    __builtin_amdgcn_raw_buffer_store_b128(dsl, rp, lane < 8 ? 4096 + lane * 16 : 0x10000, 0, 16);
  }
  asm volatile("" ::: "memory");
#pragma unroll
  for (int ks = 0; ks < 4; ++ks) qraw[1][ks] = __builtin_amdgcn_raw_buffer_load_b128(rq, foff + 256 + ks * 64, 0, 0);
  asm volatile("" ::: "memory");
  u32x4 qh[2][4];
#pragma unroll
  for (int ks = 0; ks < 4; ++ks)
#pragma unroll
    for (int j = 0; j < 4; ++j) qh[0][ks][j] = pk(bflo(qraw[0][ks][j]) * F[0][ks][2 * j], bfhi(qraw[0][ks][j]) * F[0][ks][2 * j + 1]);
#pragma unroll
  for (int ks = 0; ks < 4; ++ks) PIN(qh[0][ks]);
  const u64 tag = mix64(epoch);
  asm volatile("s_waitcnt vmcnt(4)" ::: "memory");
  if (lane == 0 && seg < 7) __hip_atomic_store(row + seg, tag, RLX_AGENT);
#pragma unroll
  for (int ks = 0; ks < 4; ++ks)
#pragma unroll
    for (int j = 0; j < 4; ++j) qh[1][ks][j] = pk(bflo(qraw[1][ks][j]) * F[1][ks][2 * j], bfhi(qraw[1][ks][j]) * F[1][ks][2 * j + 1]);
#pragma unroll
  for (int ks = 0; ks < 4; ++ks) PIN(qh[1][ks]);
  STAMP(6);

  f32x4 o[8][2];
#pragma unroll
  for (int et = 0; et < 8; ++et) { o[et][0] = (f32x4){0.f, 0.f, 0.f, 0.f}; o[et][1] = (f32x4){0.f, 0.f, 0.f, 0.f}; }
  {
    u32 kr[2], vt[2][2];
#pragma unroll
    for (int s = 0; s < 2; ++s) kr[s] = sb + LDS_K + 16384 * c + 2048 * (i >> 2) + 64 * (4 * s + (i & 3)) + 16 * (qg ^ ((2 * (i >> 2) + s) & 3));
#pragma unroll
    for (int t = 0; t < 2; ++t)
#pragma unroll
      for (int e1 = 0; e1 < 2; ++e1) vt[t][e1] = sb + LDS_V + 16384 * c + 2048 * qg + 64 * (4 * t + a4) + 16 * ((2 * e1 + (p >> 1)) ^ ((2 * qg + t) & 3)) + 8 * (p & 1);
    for (int kb = 0; kb <= hf; ++kb) {
      const u32 ko = kb * 8192;
      f32x4 at[2][2];
#pragma unroll
      for (int s = 0; s < 2; ++s) { at[s][0] = (f32x4){0.f, 0.f, 0.f, 0.f}; at[s][1] = (f32x4){0.f, 0.f, 0.f, 0.f}; }
#pragma unroll
      for (int ks = 0; ks < 4; ++ks)
#pragma unroll
        for (int s = 0; s < 2; ++s) {
          u32x4 kf = ldsr128(kr[s] + ko + ks * 512);
          at[s][0] = mfma16(kf, qh[0][ks], at[s][0]);
          at[s][1] = mfma16(kf, qh[1][ks], at[s][1]);
        }
      const int lim = 2 * i - 8 * qg + (kb == hf ? 0 : 64);
      u32x4 pf[2];
#pragma unroll
      for (int rho = 0; rho < 2; ++rho)
#pragma unroll
        for (int s = 0; s < 2; ++s) {
          float m[4];
#pragma unroll
          for (int b = 0; b < 4; ++b) m[b] = (4 * s + b - rho <= lim) ? at[s][rho][b] : 0.f;
          pf[rho][2 * s] = pk(m[0], m[1]); pf[rho][2 * s + 1] = pk(m[2], m[3]);
        }
#pragma unroll
      for (int et = 0; et < 8; ++et) {
        u32x4 vf = trfrag(vt[0][et & 1] + ko + (et >> 1) * 512, vt[1][et & 1] + ko + (et >> 1) * 512);
        o[et][0] = mfma16(vf, pf[0], o[et][0]);
        o[et][1] = mfma16(vf, pf[1], o[et][1]);
      }
    }
  }
  STAMP(7);

#pragma unroll
  for (int r = 0; r < 16; ++r) { acc[0][r] = 0.f; acc[1][r] = 0.f; }
  if (seg > 0) {
    for (int spin = 0; spin < (1 << 20); ++spin) {
      u64 fl = __hip_atomic_load(row + (lane & 7), RLX_AGENT);
      if (__all((lane & 7) >= seg || fl == tag)) break;
      __builtin_amdgcn_s_sleep(2);
    }
    __builtin_amdgcn_fence(__ATOMIC_ACQUIRE, "wavefront");
    asm volatile("" ::: "memory");
    if (seg == 7 && lane == 0) __hip_atomic_store(row + 7, tag, RLX_AGENT);
    STAMP(8);
    const __amdgpu_buffer_rsrc_t rin = __builtin_amdgcn_make_buffer_rsrc(pubh, 0, seg * PUB_SEG + PUB_WAVE, 0x00020000);
    u32x4 dv = __builtin_amdgcn_raw_buffer_load_b128(rin, lane < 56 ? (7 - (lane >> 3)) * PUB_SEG + 4096 + (lane & 7) * 16 : 0x1000000, 0, 16);
    const u32 so = lane * 16;
    u32x4 st[7][4];
#pragma unroll
    for (int n = 0; n < 3; ++n)
#pragma unroll
      for (int u = 0; u < 4; ++u) st[n][u] = __builtin_amdgcn_raw_buffer_load_b128(rin, so + (7 - n) * PUB_SEG + u * 1024, 0, 16);
    ldsw128(sb + LDS_DW + w * 1024 + lane * 16, dv);
    const u32 dr = sb + LDS_DW + w * 1024 + h * 32;
#pragma unroll
    for (int n = 0; n < 7; ++n) {
#pragma unroll
      for (int s = 0; s < 2; ++s) {
        f32x4 f0 = ldsr128f(dr + n * 128 + s * 64), f1 = ldsr128f(dr + n * 128 + s * 64 + 16);
#pragma unroll
        for (int e2 = 0; e2 < 2; ++e2) {
          u32x4 x = st[n][e2 * 2 + s];
          acc[e2][8 * s + 0] = fmaf(acc[e2][8 * s + 0], f0[0], bflo(x[0])); acc[e2][8 * s + 1] = fmaf(acc[e2][8 * s + 1], f0[1], bfhi(x[0]));
          acc[e2][8 * s + 2] = fmaf(acc[e2][8 * s + 2], f0[2], bflo(x[1])); acc[e2][8 * s + 3] = fmaf(acc[e2][8 * s + 3], f0[3], bfhi(x[1]));
          acc[e2][8 * s + 4] = fmaf(acc[e2][8 * s + 4], f1[0], bflo(x[2])); acc[e2][8 * s + 5] = fmaf(acc[e2][8 * s + 5], f1[1], bfhi(x[2]));
          acc[e2][8 * s + 6] = fmaf(acc[e2][8 * s + 6], f1[2], bflo(x[3])); acc[e2][8 * s + 7] = fmaf(acc[e2][8 * s + 7], f1[3], bfhi(x[3]));
        }
      }
      PIN(acc[0]); PIN(acc[1]);
      if (n + 3 < 7) {
        asm volatile("" ::: "memory");
        __builtin_amdgcn_sched_barrier(0);
#pragma unroll
        for (int u = 0; u < 4; ++u) st[n + 3][u] = __builtin_amdgcn_raw_buffer_load_b128(rin, so + (4 - n) * PUB_SEG + u * 1024, 0, 16);
        __builtin_amdgcn_sched_barrier(0);
      }
    }
  }
  STAMP(9);
  barrier();
  {
    const u32 sw = sb + ((4 * eh + blk) * 4 + db) * 1024 + (h * 16 + i) * 16;
#pragma unroll
    for (int cc = 0; cc < 4; ++cc)
#pragma unroll
      for (int s = 0; s < 2; ++s) {
        f32x4 f0 = ldsr128f(tabr + (4 + cc) * 512 + s * 64), f1 = ldsr128f(tabr + (4 + cc) * 512 + s * 64 + 16);
#pragma unroll
        for (int e2 = 0; e2 < 2; ++e2) {
          u32x4 x = cc ? sv[cc ? cc - 1 : 0][e2][s] : (u32x4){0u, 0u, 0u, 0u};
          u32x4 y;
          y[0] = pk(fmaf(acc[e2][8 * s + 0], f0[0], bflo(x[0])), fmaf(acc[e2][8 * s + 1], f0[1], bfhi(x[0])));
          y[1] = pk(fmaf(acc[e2][8 * s + 2], f0[2], bflo(x[1])), fmaf(acc[e2][8 * s + 3], f0[3], bfhi(x[1])));
          y[2] = pk(fmaf(acc[e2][8 * s + 4], f1[0], bflo(x[2])), fmaf(acc[e2][8 * s + 5], f1[1], bfhi(x[2])));
          y[3] = pk(fmaf(acc[e2][8 * s + 6], f1[2], bflo(x[3])), fmaf(acc[e2][8 * s + 7], f1[3], bfhi(x[3])));
          ldsw128(sw + cc * 32768 + e2 * 8192 + s * 512, y);
        }
      }
  }
  barrier();
  STAMP(10);
  {
    const u32 sr = sb + c * 32768 + lane * 16;
    const u32 ooff = (u32)((tb + 2 * i) * 256 + (qg & 1) * 32 + (qg >> 1) * 16);
#pragma unroll
    for (int m = 0; m < 4; ++m) {
#pragma unroll
      for (int e1 = 0; e1 < 2; ++e1) {
        const int et = 2 * m + e1;
#pragma unroll
        for (int ks = 0; ks < 4; ++ks) {
          u32x4 sf = ldsr128(sr + (et * 4 + ks) * 1024);
          o[et][0] = mfma16(sf, qh[0][ks], o[et][0]);
          o[et][1] = mfma16(sf, qh[1][ks], o[et][1]);
        }
      }
#pragma unroll
      for (int rho = 0; rho < 2; ++rho) {
        u32 x0 = pk(o[2 * m][rho][0], o[2 * m][rho][1]), x1 = pk(o[2 * m][rho][2], o[2 * m][rho][3]);
        u32 y0 = pk(o[2 * m + 1][rho][0], o[2 * m + 1][rho][1]), y1 = pk(o[2 * m + 1][rho][2], o[2 * m + 1][rho][3]);
        auto s0 = __builtin_amdgcn_permlane16_swap(x0, y0, false, false);
        auto s1 = __builtin_amdgcn_permlane16_swap(x1, y1, false, false);
        __builtin_amdgcn_raw_buffer_store_b128((u32x4){s0[0], s1[0], s0[1], s1[1]}, ro, ooff + rho * 256 + m * 64, 0, 0);
      }
    }
  }
  STAMP(11);
}

extern "C" void kernel_launch(void* const* d_in, const int* in_sizes, int n_in, void* d_out, int out_size, void* d_ws, size_t ws_size,
                              hipStream_t stream) {
  (void)hipFuncSetAttribute((const void*)gla_kernel, hipFuncAttributeMaxDynamicSharedMemorySize, LDS_BYTES);
  gla_kernel<<<256, 512, LDS_BYTES, stream>>>(d_in[0], d_in[1], d_in[2], d_in[3], d_out, (char*)d_ws);
}
```

```cpp
#define DYN 0
#include <hip/hip_runtime.h>
#include <hip/hip_bf16.h>
typedef unsigned short u16;
__device__ inline float bf2f(u16 x){ return __uint_as_float(((unsigned)x)<<16); }
__global__ __launch_bounds__(128) void simple_k(const u16* q,const u16* k,const u16* v,const u16* g, __hip_bfloat16* out){
  unsigned long long lk0; asm volatile("s_memrealtime %0\n\ts_waitcnt lgkmcnt(0)" : "=s"(lk0) :: "memory");
  const int h = blockIdx.x>>3, es = blockIdx.x&7; const int d = threadIdx.x;
  __shared__ float red[2][16];
  float S[16];
  #pragma unroll
  for(int e=0;e<16;e++) S[e]=0.f;
  const size_t base=(size_t)h*2048*128;
  for(int t=0;t<2048;t++){
    size_t o=base+(size_t)t*128;
    float a=1.f/(1.f+__expf(-bf2f(g[o+d]))); float kk=bf2f(k[o+d]); float qq=bf2f(q[o+d]);
    float p[16];
    #pragma unroll
    for(int e=0;e<16;e++){ float vv=bf2f(v[o+es*16+e]); S[e]=a*S[e]+kk*vv; p[e]=qq*S[e]; }
    #pragma unroll
    for(int e=0;e<16;e++){
      #pragma unroll
      for(int m=32;m>=1;m>>=1) p[e]+=__shfl_xor(p[e],m);
    }
    if((d&63)==0){
      #pragma unroll
      for(int e=0;e<16;e++) red[d>>6][e]=p[e];
    }
    __syncthreads();
    if(d<16) out[o+es*16+d]=__float2bfloat16((red[0][d]+red[1][d])*0.08838834764831845f);
    __syncthreads();
  }
  __builtin_amdgcn_fence(__ATOMIC_RELEASE, "agent");
  { const unsigned long long until = lk0 + 340000; while (__builtin_amdgcn_s_memrealtime() < until) __builtin_amdgcn_s_sleep(1); }
}
extern "C" void kernel_launch(void* const* d_in, const int* in_sizes, int n_in, void* d_out, int out_size, void* d_ws, size_t ws_size, hipStream_t stream) {
  (void)hipFuncSetAttribute((const void*)simple_k, hipFuncAttributeMaxDynamicSharedMemorySize, 160000);
  simple_k<<<256,128,DYN,stream>>>((const u16*)d_in[0],(const u16*)d_in[1],(const u16*)d_in[2],(const u16*)d_in[3],(__hip_bfloat16*)d_out);
}
```

```cpp
#include <hip/hip_runtime.h>
#include <hip/hip_bf16.h>

typedef unsigned u32;
typedef unsigned long long u64;
typedef u32 u32x4 __attribute__((ext_vector_type(4)));
typedef u32 u32x2 __attribute__((ext_vector_type(2)));
typedef short s16x4 __attribute__((ext_vector_type(4)));
typedef __bf16 bf16x8 __attribute__((ext_vector_type(8)));
typedef __bf16 bf16x2 __attribute__((ext_vector_type(2)));
typedef float f32x2 __attribute__((ext_vector_type(2)));
typedef float f32x4 __attribute__((ext_vector_type(4)));
typedef float f32x16 __attribute__((ext_vector_type(16)));
typedef __amdgpu_buffer_rsrc_t rsrc_t;
#define LAS __attribute__((address_space(3)))
#define DI __device__ __forceinline__
#define PIN(x) asm volatile("" : "+v"(x))
#define SB() __builtin_amdgcn_sched_barrier(0)
#define FENCE() do { asm volatile("" ::: "memory"); SB(); } while (0)
#define WAITV(n) asm volatile("s_waitcnt vmcnt(" #n ")" ::: "memory")
#define WAITR(n, x) asm volatile("s_waitcnt vmcnt(" #n ")" : "+v"(x) :: "memory")

constexpr int kT = 2048, kSEG = 256;
constexpr int R_K = 0, R_V = 65536, R_X = 131072;
constexpr int LDS_BYTES = 163840;
constexpr float kLim = 0x1p88f;
constexpr int WS_PUB = 1 << 20;
constexpr int PUB_WAVE = 4096 + 128;
constexpr int PUB_SEG = 8 * PUB_WAVE;
constexpr int PUB_HEAD = 8 * PUB_SEG;

DI float bflo(u32 x) { return __uint_as_float(x << 16); }
DI float bfhi(u32 x) { return __uint_as_float(x & 0xffff0000u); }
DI f32x2 up2(u32 x) { return (f32x2){bflo(x), bfhi(x)}; }
DI u32 pk2(f32x2 f) { return __builtin_bit_cast(u32, __builtin_convertvector(f, bf16x2)); }
DI f32x16 mfma32(u32x4 a, u32x4 b, f32x16 c) { return __builtin_amdgcn_mfma_f32_32x32x16_bf16(__builtin_bit_cast(bf16x8, a), __builtin_bit_cast(bf16x8, b), c, 0, 0, 0); }
DI u32x2 trrd(u32 addr) { return __builtin_bit_cast(u32x2, __builtin_amdgcn_ds_read_tr16_b64_v4i16((LAS s16x4*)(size_t)addr)); }
DI u32x4 trfrag(u32 a0, u32 a1) { u32x2 x = trrd(a0), y = trrd(a1); return (u32x4){x[0], x[1], y[0], y[1]}; }
DI u32x4 ldsr128(u32 addr) { return *(LAS u32x4*)(size_t)addr; }
DI void ldsw128(u32 addr, u32x4 v) { *(LAS u32x4*)(size_t)addr = v; }
DI void barrier() { asm volatile("s_waitcnt lgkmcnt(0)\n\ts_barrier" ::: "memory"); }
DI u64 mix64(u64 z) { z += 0x9E3779B97F4A7C15ull; z = (z ^ (z >> 30)) * 0xBF58476D1CE4E5B9ull; z = (z ^ (z >> 27)) * 0x94D049BB133111EBull; return z ^ (z >> 31); }
DI f32x2 sub2(const f32x16& a, int i) { return (f32x2){a[2 * i], a[2 * i + 1]}; }
DI u32x4 pack8(const f32x16& a, int s) { return (u32x4){pk2(sub2(a, 4 * s)), pk2(sub2(a, 4 * s + 1)), pk2(sub2(a, 4 * s + 2)), pk2(sub2(a, 4 * s + 3))}; }
DI u32x4 pack8t(const f32x16& a, int s) {
  u32x4 o;
#pragma unroll
  for (int i = 0; i < 2; ++i) {
    const u32 t = pk2((f32x2){a[8 * s + 4 * i], a[8 * s + 4 * i + 2]});
    o[2 * i] = __builtin_amdgcn_perm(__float_as_uint(a[8 * s + 4 * i + 1]), t, 0x07060100u);
    o[2 * i + 1] = __builtin_amdgcn_perm(__float_as_uint(a[8 * s + 4 * i + 3]), t, 0x07060302u);
  }
  return o;
}
DI f32x2 lo2(u32 a, u32 b) { return (f32x2){bflo(a), bflo(b)}; }
DI f32x2 hi2(u32 a, u32 b) { return (f32x2){__uint_as_float(a), __uint_as_float(b)}; }
DI f32x2 bc2(float x) { return (f32x2){x, x}; }
DI f32x2 fma2(f32x2 a, f32x2 b, f32x2 c) { return __builtin_elementwise_fma(a, b, c); }
DI f32x16 zero16() { f32x16 z; _Pragma("unroll") for (int i = 0; i < 16; ++i) z[i] = 0.f; return z; }


constexpr int PFLAG1 = 24;
constexpr int NPOLL = 6;
constexpr int kUpto[29] = {4, 5,6,7,8,9,10,11,12, 13,14,15,16,17,18,19,20, 24,26,28,30, 31,32,32,32,32,32,32,32};
enum { OP_MISC = 100, OP_PUB0, OP_PUB1, MK_BEGIN = 200, MK_END = 300 };
struct Seq {
  int id[160]; int n;
  constexpr Seq() : id(), n(0) {
    int issued = 0;
    for (int p = 0; p < 29; ++p) {
      id[n++] = MK_BEGIN + p;
      if (p == 18) { id[n++] = OP_MISC; id[n++] = OP_MISC; id[n++] = OP_PUB0; }
      if (p == 20) { id[n++] = OP_MISC; id[n++] = OP_MISC; id[n++] = OP_PUB1; }
      if (p == 21 + NPOLL) id[n++] = OP_MISC;
      id[n++] = MK_END + p;
      if (p == PFLAG1) id[n++] = OP_MISC;
      for (; issued < kUpto[p]; ++issued) id[n++] = issued;
      if (p == 0) { id[n++] = OP_MISC; id[n++] = OP_MISC; id[n++] = OP_MISC; }
    }
  }
  constexpr int younger(int op, int mk) const {
    int c = -1;
    for (int i = 0; i < n && id[i] != mk; ++i) { if (id[i] == op) c = 0; else if (c >= 0 && id[i] < MK_BEGIN) ++c; }
    return c;
  }
};
constexpr Seq kSeq;
template <int OP, int MK> DI void waitv() { constexpr int n = kSeq.younger(OP, MK); static_assert(n >= 0 && n < 64, "schedule"); asm volatile("s_waitcnt vmcnt(%0)" :: "i"(n) : "memory"); }
template <int OP, int MK, class T> DI void waitr(T& x) { constexpr int n = kSeq.younger(OP, MK); static_assert(n >= 0 && n < 64, "schedule"); asm volatile("s_waitcnt vmcnt(%1)" : "+v"(x) : "i"(n) : "memory"); }

struct Ctx {
  rsrc_t rg, rk, rq, rv;
  u32 poff, voff;
  int sstep, s0;
  u32 vlds;
  u32 t0;
};
template <int N>
DI void issue(const Ctx& c, u32x4 (&L)[32]) {
  if constexpr (N < 32) {
    SB();
    if constexpr (N >= 6 && N < 20 && (N % 2) == 0) {
      while ((u32)__builtin_amdgcn_s_memrealtime() - c.t0 < (u32)(170 + (N - 4) * 30)) __builtin_amdgcn_s_sleep(1);
      SB();
    }
    if constexpr (N >= 16 && N < 24) {
      asm volatile("s_mov_b32 m0, %0\n\ts_nop 0\n\tbuffer_load_dwordx4 %1, %2, %3 offen nt lds"
                   :: "s"(c.vlds + ((N >> 2) & 1) * 32768 + (N & 3) * 4096), "v"(c.voff), "s"(c.rv), "s"((N & 3) * 8192 + ((N >> 2) & 1) * 128) : "memory");
    } else {
      asm volatile("buffer_load_dwordx4 %0, %1, %2, %3 offen nt"
                   : "=v"(L[N]) : "v"(c.poff), "s"(N < 8 ? c.rg : (N < 16 ? c.rk : c.rq)), "s"(c.s0 + (N & 7) * c.sstep) : "memory");
    }
    SB();
  }
}

template <int A, int B>
DI void issue_range(const Ctx& c, u32x4 (&L)[32]) { if constexpr (A < B) { issue<A>(c, L); issue_range<A + 1, B>(c, L); } }
template <int P>
DI void issue_at(const Ctx& c, u32x4 (&L)[32]) { issue_range<(P ? kUpto[P ? P - 1 : 0] : 0), kUpto[P]>(c, L); }

template <int N>
DI void gstep(const Ctx& c, u32x4 (&L)[32], f32x2 (&P)[9][4]) {
  waitr<N, MK_BEGIN + 1 + N>(L[N]);
#pragma unroll
  for (int j = 0; j < 4; ++j) {
    f32x2 x = up2(L[N][j]) * -1.4426950408889634f;
    const f32x2 ex = {__builtin_amdgcn_exp2f(x[0]), __builtin_amdgcn_exp2f(x[1])};
    P[N + 1][j] = N ? fma2(P[N][j], ex, P[N][j]) : ex + 1.0f;
    PIN(P[N + 1][j]);
  }
  issue_at<1 + N>(c, L);
}
template <int UP, int N>
DI void kcomp(u32x4 (&L)[32], f32x2 (&P)[9][4], const f32x2 (&cy)[4], f32x2 (&Fq)[8][4], u32 kw0, u32 kw1) {
  const float scale = 0.08838834764831845f;
  u32x4 o;
#pragma unroll
  for (int j = 0; j < 4; ++j) {
    f32x2 f = (UP || N) ? P[UP ? N + 1 : N][j] * cy[j] : cy[j];
    f32x2 fi = (f32x2){__builtin_amdgcn_rcpf(f[0]), __builtin_amdgcn_rcpf(f[1])};
    o[j] = pk2(up2(L[8 + N][j]) * (UP ? f : fi));
    Fq[N][j] = (UP ? fi : f) * scale;
    PIN(Fq[N][j]);
  }
  constexpr int r8 = UP ? N : 7 - N;
  ldsw128((((r8 >> 2) ^ (r8 >> 1)) & 1 ? kw1 : kw0) + 64 * r8, o);
}
template <int N>
DI void kstep(int hf, u32x4 (&L)[32], f32x2 (&P)[9][4], const f32x2 (&cy)[4], f32x2 (&Fq)[8][4], u32 kw0, u32 kw1) {
  waitr<8 + N, MK_BEGIN + 9 + N>(L[8 + N]);
  if (hf) kcomp<1, N>(L, P, cy, Fq, kw0, kw1); else kcomp<0, N>(L, P, cy, Fq, kw0, kw1);
}
template <int N>
DI void qstep(u32x4 (&L)[32], const f32x2 (&Fq)[8][4], u32 qw) {
  u32x4 o;
#pragma unroll
  for (int j = 0; j < 4; ++j) o[j] = pk2(up2(L[24 + N][j]) * Fq[N][j]);
  ldsw128((qw ^ ((N & 3) << 4)) + (N & 3) * 256, o);
}
template <int V> struct IC { static constexpr int value = V; };
struct Inc { u32x4 u[7][2]; u32 dr[3]; float dv[8]; };
struct Hand { rsrc_t rp, rin, rrow; char* row; u32 lane16, doff; u32x2 tag; int seg; };
template <int X> DI void pub_tile(const Hand& hd, u32x4 s0, u32x4 s1, float dseg) {
  __builtin_amdgcn_raw_buffer_store_b128(s0, hd.rp, hd.lane16 + X * 2048, 0, 16);
  __builtin_amdgcn_raw_buffer_store_b128(s1, hd.rp, hd.lane16 + X * 2048 + 1024, 0, 16);
  if (X == 0) __builtin_amdgcn_raw_buffer_store_b32(__float_as_uint(dseg), hd.rp, 4096 + (hd.lane16 >> 2), 0, 16);
  FENCE();
}
DI void put_tag(const Hand& hd, int off, bool on) {
  __builtin_amdgcn_raw_buffer_store_b64(hd.tag, __builtin_amdgcn_make_buffer_rsrc(hd.row + off, 0, on ? 8 : 0, 0x00020000), hd.lane16, 0, 16);
  FENCE();
}
template <int X> DI void poll(u32x2& fl, const Hand& hd) {
  asm volatile("buffer_load_dwordx2 %0, %1, %2, 0 offen offset:%3 sc1" : "=v"(fl) : "v"(hd.lane16 & 112), "s"(hd.rrow), "i"(128 * X) : "memory");
}
template <int X> DI void await(u32x2& fl, const Hand& hd) {
  int spin = 0;
#pragma nounroll
  while (!__all((hd.lane16 & 112) >= 16u * hd.seg || (fl[0] == hd.tag[0] && fl[1] == hd.tag[1])) && ++spin < (1 << 18)) {
    __builtin_amdgcn_s_sleep(1);
    poll<X>(fl, hd);
    WAITR(0, fl);
  }
  FENCE();
}
template <int X, int M>
DI void load_inc_(Inc& in, const Hand& hd) {
  if constexpr (M <= 7) {
    if (hd.seg >= M) {
      asm volatile("buffer_load_dwordx4 %0, %1, %2, %3 offen sc1" : "=v"(in.u[M - 1][0]) : "v"(hd.lane16), "s"(hd.rin), "s"(M * PUB_SEG + X * 2048) : "memory");
      asm volatile("buffer_load_dwordx4 %0, %1, %2, %3 offen offset:1024 sc1" : "=v"(in.u[M - 1][1]) : "v"(hd.lane16), "s"(hd.rin), "s"(M * PUB_SEG + X * 2048) : "memory");
      load_inc_<X, M + 1>(in, hd);
    }
  }
}
template <int X> DI void load_inc(Inc& in, const Hand& hd) {
  SB();
  if (X == 0) {
#pragma unroll
    for (int i = 0; i < 3; ++i)
      asm volatile("buffer_load_dword %0, %1, %2, 0 offen sc1" : "=v"(in.dr[i]) : "v"(hd.doff + 2 * i * PUB_SEG), "s"(hd.rin) : "memory");
  }
  load_inc_<X, 1>(in, hd);
  SB();
}
template <int M>
DI void accum_(Inc& in, const Hand& hd, float wgt, f32x2 (&lo)[4], f32x2 (&hi)[4]) {
  if constexpr (M <= 7) {
    if (hd.seg >= M) {
      PIN(in.u[M - 1][0]); PIN(in.u[M - 1][1]);
      const f32x2 d = bc2(wgt);
#pragma unroll
      for (int i = 0; i < 4; ++i) {
        const u32 a = in.u[M - 1][i >> 1][2 * (i & 1)], b = in.u[M - 1][i >> 1][2 * (i & 1) + 1];
        lo[i] = M > 1 ? fma2(lo2(a, b), d, lo[i]) : lo2(a, b); hi[i] = M > 1 ? fma2(hi2(a, b), d, hi[i]) : hi2(a, b);
        PIN(lo[i]); PIN(hi[i]);
      }
      accum_<M + 1>(in, hd, M > 1 ? wgt * in.dv[M] : in.dv[M], lo, hi);
    }
  }
}
template <int X>
DI void gather(Inc& in, const Hand& hd, f32x2 (&lo)[4], f32x2 (&hi)[4]) {
  WAITV(0);
  if (X == 0) {
#pragma unroll
    for (int i = 0; i < 3; ++i) {
      PIN(in.dr[i]);
      auto sw = __builtin_amdgcn_permlane32_swap(in.dr[i], in.dr[i], false, false);
      in.dv[2 * i + 1] = __uint_as_float(sw[0]); in.dv[2 * i + 2] = __uint_as_float(sw[1]);
    }
    in.dv[7] = 0.f;
  }
#pragma unroll
  for (int i = 0; i < 4; ++i) { lo[i] = (f32x2){0.f, 0.f}; hi[i] = (f32x2){0.f, 0.f}; }
  accum_<1>(in, hd, 1.0f, lo, hi);
}
DI void fold1(int cc, int i, const f32x2 (&lo)[4], const f32x2 (&hi)[4], const float (&phi)[4], u32x4 (&sv)[4][2]) {
  const f32x2 f = bc2(phi[cc]);
  const u32 a = sv[cc][i >> 1][2 * (i & 1)], b = sv[cc][i >> 1][2 * (i & 1) + 1];
  const f32x2 l = cc ? fma2(lo[i], f, lo2(a, b)) : lo[i] * f, g = cc ? fma2(hi[i], f, hi2(a, b)) : hi[i] * f;
  sv[cc][i >> 1][2 * (i & 1)] = pk2((f32x2){l[0], g[0]});
  sv[cc][i >> 1][2 * (i & 1) + 1] = pk2((f32x2){l[1], g[1]});
}

DI void slow_path(const Ctx& cx, const Hand& hd, rsrc_t ro, u32 sb, int tid, int db, int eq, int h, int rs) {
  LAS float* stg = (LAS float*)(size_t)sb;
  LAS float* buf = (LAS float*)(size_t)(sb + 65536);
  LAS float* dtot = (LAS float*)(size_t)(sb + 131072);
  const int e = tid & 127, dq = tid >> 7;
  WAITV(0);
  float S[32], dprod = 1.0f;
#pragma unroll
  for (int i = 0; i < 32; ++i) S[i] = 0.f;
#pragma nounroll
  for (int pass = 0; pass < 2; ++pass) {
#pragma nounroll
    for (int t0 = 0; t0 < kSEG; t0 += 32) {
      __syncthreads();
#pragma unroll
      for (int ar = 0; ar < 4; ++ar) {
        const u32x4 raw = __builtin_amdgcn_raw_buffer_load_b128(ar == 0 ? cx.rg : (ar == 1 ? cx.rk : (ar == 2 ? cx.rq : cx.rv)), tid * 16, t0 * 256, 0);
#pragma unroll
        for (int j = 0; j < 8; ++j) {
          float x = (j & 1) ? bfhi(raw[j >> 1]) : bflo(raw[j >> 1]);
          if (ar == 0) x = __builtin_amdgcn_rcpf(1.0f + __builtin_amdgcn_exp2f(x * -1.4426950408889634f));
          stg[ar * 4096 + tid * 8 + j] = x;
        }
      }
      __syncthreads();
#pragma nounroll
      for (int t = 0; t < 32; ++t) {
        const float vv = stg[3 * 4096 + t * 128 + e];
        float acc = 0.f;
#pragma unroll
        for (int i = 0; i < 32; ++i) {
          const int ix = t * 128 + dq * 32 + i;
          S[i] = fmaf(stg[ix], S[i], stg[4096 + ix] * vv);
          acc = fmaf(stg[8192 + ix], S[i], acc);
          if ((i & 7) == 7) SB();
        }
        buf[dq * 4096 + t * 128 + e] = acc;
        dprod *= stg[t * 128 + e];
      }
      if (pass) {
        __syncthreads();
        u32x4 o;
#pragma unroll
        for (int j = 0; j < 4; ++j) {
          f32x2 y;
#pragma unroll
          for (int u = 0; u < 2; ++u) { const int ix = tid * 8 + 2 * j + u; y[u] = ((buf[ix] + buf[4096 + ix]) + (buf[8192 + ix] + buf[12288 + ix])) * 0.08838834764831845f; }
          o[j] = pk2(y);
        }
        __builtin_amdgcn_raw_buffer_store_b128(o, ro, tid * 16, t0 * 256, 0);
      }
    }
    if (pass == 0) {
      __syncthreads();
#pragma unroll
      for (int i = 0; i < 32; ++i) buf[(dq * 32 + i) * 128 + e] = S[i];
      if (dq == 0) dtot[e] = dprod;
      __syncthreads();
      LAS float* mine = buf + (32 * db + rs) * 128 + 32 * eq + 8 * h;
      f32x16 tl;
#pragma unroll
      for (int i = 0; i < 16; ++i) tl[i] = mine[16 * (i >> 3) + (i & 7)];
      pub_tile<0>(hd, pack8t(tl, 0), pack8t(tl, 1), dtot[32 * db + rs]);
#pragma unroll
      for (int i = 0; i < 16; ++i) tl[i] = mine[64 + 16 * (i >> 3) + (i & 7)];
      pub_tile<1>(hd, pack8t(tl, 0), pack8t(tl, 1), 0.f);
      WAITV(0);
      put_tag(hd, hd.seg * 16, hd.seg < 7); put_tag(hd, hd.seg * 16 + 128, hd.seg < 7);
      u32x2 fl;
      poll<0>(fl, hd); WAITR(0, fl); await<0>(fl, hd);
      poll<1>(fl, hd); WAITR(0, fl); await<1>(fl, hd);
      put_tag(hd, 112, hd.seg == 7);
#pragma nounroll
      for (int u = 0; u < 4; ++u) {
        float acc[8], wgt = 1.0f;
#pragma unroll
        for (int j = 0; j < 8; ++j) acc[j] = 0.f;
#pragma nounroll
        for (int m = 1; m <= hd.seg; ++m) {
          const u32x4 z = __builtin_amdgcn_raw_buffer_load_b128(hd.rin, hd.lane16 + u * 1024 + m * PUB_SEG, 0, 16);
          const float dm = __uint_as_float(__builtin_amdgcn_raw_buffer_load_b32(hd.rin, 4096 + ((hd.lane16 >> 2) & 127) + m * PUB_SEG, 0, 16));
#pragma unroll
          for (int j = 0; j < 4; ++j) { acc[2 * j] = fmaf(bflo(z[j]), wgt, acc[2 * j]); acc[2 * j + 1] = fmaf(__uint_as_float(z[j]), wgt, acc[2 * j + 1]); }
          wgt *= dm;
        }
#pragma unroll
        for (int j = 0; j < 8; ++j) mine[64 * (u >> 1) + 16 * (u & 1) + j] = acc[j];
      }
      __syncthreads();
#pragma unroll
      for (int i = 0; i < 32; ++i) S[i] = buf[(dq * 32 + i) * 128 + e];
    }
  }
}

__global__ __launch_bounds__(512, 2) void gla_kernel(const void* __restrict__ qp, const void* __restrict__ kp, const void* __restrict__ vp,
                                                     const void* __restrict__ gp, void* __restrict__ op, char* ws) {
  extern __shared__ __attribute__((aligned(16))) char smem[];
  const u32 sb = (u32)(size_t)(LAS char*)smem;
  const int tid = threadIdx.x, lane = tid & 63, w = __builtin_amdgcn_readfirstlane(tid >> 6);
  const int bid = blockIdx.x, head = (bid & 7) * 4 + (bid >> 6), seg = (bid >> 3) & 7;
  const int c = w & 3, cls = w >> 2, hf = cls ^ 1, tb = 64 * c + 32 * hf;
  const int dg = lane & 15, tg = lane >> 4;
  const size_t gbase = ((size_t)head * kT + (size_t)seg * kSEG) * 256;
  Ctx cx;
  cx.t0 = (u32)__builtin_amdgcn_s_memrealtime() - 12 * seg;
  cx.rq = __builtin_amdgcn_make_buffer_rsrc((char*)qp + gbase, 0, 65536, 0x00020000);
  cx.rk = __builtin_amdgcn_make_buffer_rsrc((char*)kp + gbase, 0, 65536, 0x00020000);
  cx.rv = __builtin_amdgcn_make_buffer_rsrc((char*)vp + gbase, 0, 65536, 0x00020000);
  cx.rg = __builtin_amdgcn_make_buffer_rsrc((char*)gp + gbase, 0, 65536, 0x00020000);
  const int R8 = (tb >> 3) + (hf ? tg : 3 - tg);
  cx.poff = (u32)(R8 * 2048 + dg * 16);
  cx.s0 = hf ? 0 : 7 * 256; cx.sstep = hf ? 256 : -256;
  cx.vlds = sb + R_V + (16 * cls + c) * 1024;
  Hand hd;
  hd.row = ws + (head * 8 + w) * 256;
  hd.rrow = __builtin_amdgcn_make_buffer_rsrc(hd.row, 0, 256, 0x00020000);
  u32 lane16 = lane * 16;
  u32x2 ep2;
  u32x4 L[32];
  asm volatile("s_nop 4");
  issue_at<0>(cx, L);
  u32 pf1;
  {
    const char* base = w == 0 ? (const char*)kp + gbase : w == 1 ? (const char*)vp + gbase : w == 2 ? (const char*)qp + gbase : w == 3 ? (const char*)op + gbase
                       : ws + WS_PUB + (size_t)head * PUB_HEAD + (w - 4) * (PUB_HEAD / 4);
    const rsrc_t rt = __builtin_amdgcn_make_buffer_rsrc((char*)base, 0, 65536, 0x00020000);
    asm volatile("s_nop 4\n\tbuffer_load_dword %0, %1, %2, 0 offen sc1" : "=v"(pf1) : "v"(lane * 65536), "s"(rt) : "memory");
  }
  u32 pf0;
  {
    u64 pc; u32 len;
    asm volatile("s_getpc_b64 %0\n.Lgla_pc:\n\ts_mov_b32 %1, .Lgla_end-.Lgla_pc" : "=s"(pc), "=s"(len));
    const rsrc_t rc = __builtin_amdgcn_make_buffer_rsrc((char*)pc, 0, len, 0x00020000);
    asm volatile("buffer_load_dword %0, %1, %2, 0 offen" : "=v"(pf0) : "v"(((lane & 31) * 8 + w) * 128 + (lane >> 5) * 0x1000000), "s"(rc) : "memory");
  }
  asm volatile("buffer_load_dwordx2 %0, %1, %2, 0 offen sc1" : "=v"(ep2) : "v"(lane16 & 112), "s"(hd.rrow) : "memory");
  char* pubh = ws + WS_PUB + (size_t)head * PUB_HEAD + (7 - seg) * PUB_SEG + w * PUB_WAVE;
  hd.rp = __builtin_amdgcn_make_buffer_rsrc(pubh, 0, seg < 7 ? PUB_WAVE : 0, 0x00020000);
  hd.rin = __builtin_amdgcn_make_buffer_rsrc(pubh, 0, seg * PUB_SEG + PUB_WAVE, 0x00020000);
  hd.seg = seg;
  const rsrc_t ro = __builtin_amdgcn_make_buffer_rsrc((char*)op + gbase, 0, 65536, 0x00020000);
  const int db = w >> 1, eq = w & 1, h = lane >> 5, r = lane & 31;
  const int rs = (r & 19) | ((r & 4) << 1) | ((r & 8) >> 1);
  {
    const int vr = (lane >> 2) & 7;
    cx.voff = (u32)((128 * cls + 8 * c + vr) * 256 + h * 64 + (((lane & 3) ^ ((2 * c + (vr >> 2)) & 3) ^ ((vr >> 1) & 1)) * 16));
  }
  u32 tro0, tro1;
  {
    const int blk = (lane >> 4) & 1, a4 = (lane & 15) >> 2, p = lane & 3;
    tro0 = sb + 1024 * h + 64 * a4 + 16 * ((2 * blk + (p & 1)) ^ (2 * h) ^ (a4 >> 1)) + 8 * (p >> 1);
    tro1 = sb + 1024 * h + 64 * (4 + a4) + 16 * ((2 * blk + (p & 1)) ^ (2 * h + 1) ^ (a4 >> 1)) + 8 * (p >> 1);
  }
  u32 rw0 = 64 * (rs & 7) + 16 * (h ^ ((rs >> 2) & 3) ^ ((rs >> 1) & 1));
  PIN(cx.voff); PIN(tro0); PIN(tro1); PIN(rw0); PIN(lane16);
  hd.lane16 = lane16; hd.doff = (1 + h) * PUB_SEG + 4096 + r * 4;
  SB();

  f32x2 P[9][4], cy[4];
  gstep<0>(cx, L, P); gstep<1>(cx, L, P); gstep<2>(cx, L, P); gstep<3>(cx, L, P);
  gstep<4>(cx, L, P); gstep<5>(cx, L, P); gstep<6>(cx, L, P); gstep<7>(cx, L, P);
  PIN(ep2); PIN(pf0);
  PIN(pf1);
  const u64 tag64 = mix64(((u64)(u32)__builtin_amdgcn_readlane(ep2[1], 7) << 32) | (u32)__builtin_amdgcn_readlane(ep2[0], 7));
  hd.tag = (u32x2){(u32)tag64, (u32)(tag64 >> 32)};
  if (w >= 4) __builtin_amdgcn_s_setprio(1);
  const u32 xw = sb + R_X + w * 4096;
  {
    f32x2 tot[4];
#pragma unroll
    for (int j = 0; j < 4; ++j)
#pragma unroll
      for (int e = 0; e < 2; ++e) {
        const u32 t = __float_as_uint(P[8][j][e]);
        auto s1 = __builtin_amdgcn_permlane16_swap(t, t, false, false);
        const float a = __uint_as_float(s1[0]), pr = a * __uint_as_float(s1[1]);
        auto s2 = __builtin_amdgcn_permlane32_swap(0x3f800000u, __float_as_uint(pr), false, false);
        const float x = __uint_as_float(s2[0]);
        tot[j][e] = x * __uint_as_float(s2[1]);
        cy[j][e] = (tg & 1) ? x * a : x;
      }
    bool bad = false;
#pragma unroll
    for (int j = 0; j < 4; ++j) bad |= !(tot[j][0] <= kLim) || !(tot[j][1] <= kLim);
    const u32 anybad = __any(bad);
    if (tg == 3) {
      *(LAS u32*)(size_t)(xw + 512) = anybad;
      *(LAS f32x4*)(size_t)(xw + dg * 32) = (f32x4){tot[0][0], tot[0][1], tot[1][0], tot[1][1]};
      *(LAS f32x4*)(size_t)(xw + dg * 32 + 16) = (f32x4){tot[2][0], tot[2][1], tot[3][0], tot[3][1]};
    }
  }
  f32x2 Fq[8][4];
  {
    const u32 kb = sb + R_K + 2048 * R8 + 512 * (dg >> 2);
    const u32 kw0 = kb + 16 * ((dg & 3) ^ ((2 * R8) & 3)), kw1 = kb + 16 * ((dg & 3) ^ ((2 * R8 + 1) & 3));
    auto kpoint = [&](auto NC) {
      constexpr int N = decltype(NC)::value, PT = 9 + N;
      kstep<N>(hf, L, P, cy, Fq, kw0, kw1);
      issue_at<PT>(cx, L);
    };
    kpoint(IC<0>{}); kpoint(IC<1>{}); kpoint(IC<2>{}); kpoint(IC<3>{}); kpoint(IC<4>{}); kpoint(IC<5>{}); kpoint(IC<6>{}); kpoint(IC<7>{});
  }
  __builtin_amdgcn_s_setprio(0);

  float fu[4], fl_[4], phi[4];
  u32x4 sv[2][4][2];
  {
    const u32 ka0 = tro0 + R_K + 1024 * h + 512 * db, ka1 = tro1 + R_K + 1024 * h + 512 * db;
    const u32 va0 = tro0 + R_V + 512 * eq, va1 = tro1 + R_V + 512 * eq;
    u32x4 kf[8];
    f32x16 a, b;
    barrier();
    {
#pragma unroll
      for (int k = 0; k < 8; ++k) kf[k] = trfrag(ka0 + k * 4096, ka1 + k * 4096);
      float f[8];
      const u32 badw = *(LAS u32*)(size_t)(sb + R_X + (lane & 7) * 4096 + 512);
#pragma unroll
      for (int i = 0; i < 8; ++i) f[i] = __builtin_amdgcn_rcpf(*(LAS float*)(size_t)(sb + R_X + i * 4096 + (32 * db + rs) * 4));
#pragma unroll
      for (int i = 0; i < 4; ++i) { fu[i] = f[i]; fl_[i] = f[4 + i]; phi[i] = i ? phi[i - 1] * f[i - 1] * f[4 + i] : f[4]; }
      PIN(fu[0]); PIN(fu[1]); PIN(fu[2]); PIN(fu[3]); PIN(fl_[1]); PIN(fl_[2]); PIN(fl_[3]); PIN(phi[0]); PIN(phi[1]); PIN(phi[2]); PIN(phi[3]);
      if (__builtin_expect(__any(badw != 0), 0)) { slow_path(cx, hd, ro, sb, tid, db, eq, h, rs); return; }
    }
#pragma unroll
    for (int sg = 0; sg < 4; ++sg) {
      const int x = sg >> 1, cp = sg & 1;
      if (cls == cp) { if (sg == 0) waitv<19, MK_BEGIN + 17>(); if (sg == 1) waitv<19, MK_BEGIN + 18>(); if (sg == 2) waitv<23, MK_BEGIN + 19>(); if (sg == 3) waitv<23, MK_BEGIN + 20>(); }
      barrier();
      u32x4 vf[8];
#pragma unroll
      for (int k = 0; k < 8; ++k) {
        const int kk = (k >> 1) + 4 * ((k & 1) ^ 1);
        vf[kk] = trfrag(va0 + x * 32768 + (8 * cp + kk) * 2048, va1 + x * 32768 + (8 * cp + kk) * 2048);
        SB();
      }
      if (cp == 0) a = zero16();
      else {
        a = (a * fu[0]) * fl_[1];
        sv[x][1][0] = pack8t(a, 0); sv[x][1][1] = pack8t(a, 1); PIN(sv[x][1][0]); PIN(sv[x][1][1]);
        a = ((a + b) * fu[1]) * fl_[2];
        sv[x][2][0] = pack8t(a, 0); sv[x][2][1] = pack8t(a, 1); PIN(sv[x][2][0]); PIN(sv[x][2][1]);
        SB();
      }
      b = zero16();
#pragma unroll
      for (int k = 0; k < 4; ++k) {
        b = mfma32(vf[4 + k], kf[4 + k], b);
        a = mfma32(vf[k], kf[k], a);
        if (sg < 3) {
          kf[k] = trfrag(ka0 + (8 * (cp ^ 1) + k) * 4096, ka1 + (8 * (cp ^ 1) + k) * 4096);
          kf[4 + k] = trfrag(ka0 + (8 * (cp ^ 1) + 4 + k) * 4096, ka1 + (8 * (cp ^ 1) + 4 + k) * 4096);
        }
        SB();
      }
      if (sg == 3) { waitv<OP_PUB0, MK_BEGIN + 20>(); put_tag(hd, seg * 16, seg < 7); }
      if (cp == 1) {
        __builtin_amdgcn_s_setprio(2);
        a = (a * fu[2]) * fl_[3];
        b = (a + b) * fu[3];
        if (x == 0) pub_tile<0>(hd, pack8t(b, 0), pack8t(b, 1), phi[3] * fu[3]); else pub_tile<1>(hd, pack8t(b, 0), pack8t(b, 1), 0.f);
        __builtin_amdgcn_s_setprio(0);
        sv[x][3][0] = pack8t(a, 0); sv[x][3][1] = pack8t(a, 1); PIN(sv[x][3][0]); PIN(sv[x][3][1]);
      }
      if (sg == 0) issue_at<17>(cx, L); if (sg == 1) issue_at<18>(cx, L); if (sg == 2) issue_at<19>(cx, L);
    }
  }

  u32x4 qh[8];
  Inc in;
  issue_at<20>(cx, L);
  u32 k0 = sb + R_K + 16384 * c + 2048 * (rs >> 3) + rw0;
  u32 iw0 = sb + 1024 * (4 * db + (rs >> 3)) + 512 * eq + rw0;
  u32 ow = xw + r * 128 + 16 * (h ^ (r & 7));
  u32 ooff = (u32)((tb + (hf ? (lane >> 3) : 7 - (lane >> 3))) * 256 + 16 * ((lane & 7) ^ (lane >> 3)));
  PIN(ow);
  PIN(k0); PIN(iw0); PIN(ooff); PIN(hd.doff);
  u32x4 kr0[8];
  u32x2 fl;
  {
    const u32 qw = xw + tg * 1024 + 16 * (dg ^ (tg << 2));
    const int qrow = 4 * (r >> 3) + (r & 3);
    const u32 qr = xw + qrow * 256 + 16 * (h ^ qrow);
    auto qpoint = [&](auto NC) {
      constexpr int N = decltype(NC)::value, PT = 21 + N;
      waitr<24 + N, MK_BEGIN + PT>(L[24 + N]);
      if constexpr (N == NPOLL) poll<0>(fl, hd);
      qstep<N>(L, Fq, qw);
      if constexpr (N == 3) {
        if (!(r & 4)) {
#pragma unroll
          for (int ks = 0; ks < 8; ++ks) qh[ks] = ldsr128(qr ^ (ks << 5));
        }
      }
      if constexpr (N == 6) {
#pragma unroll
        for (int ks = 0; ks < 8; ++ks) { kr0[ks] = ldsr128((ks & 1 ? k0 ^ 32 : k0) + hf * 8192 + (ks >> 1) * 512); }
        SB();
      }
      if constexpr (N == 7) {
        if (r & 4) {
#pragma unroll
          for (int ks = 0; ks < 8; ++ks) qh[ks] = ldsr128(qr ^ (ks << 5));
        }
      }
      if constexpr (PT == PFLAG1) { waitv<OP_PUB1, MK_END + PT>(); put_tag(hd, seg * 16 + 128, seg < 7); }

      issue_at<PT>(cx, L);
    };
    qpoint(IC<0>{}); qpoint(IC<1>{}); qpoint(IC<2>{}); qpoint(IC<3>{}); qpoint(IC<4>{}); qpoint(IC<5>{}); qpoint(IC<6>{}); qpoint(IC<7>{});
  }
  WAITR(0, fl);
  await<0>(fl, hd);
  load_inc<0>(in, hd);
  poll<1>(fl, hd);
  u32x4 pf[2][2];
  {
    const u32 k1 = k0 ^ 32;
    {
      f32x16 at = zero16();
#pragma unroll
      for (int ks = 0; ks < 8; ++ks) at = mfma32(kr0[ks], qh[ks], at);
#pragma unroll
      for (int i = 0; i < 16; ++i) {
        const int v = 16 * (i >> 3) + (i & 7);
        const u64 mu = ((u64)(0xffffffffu << (v + 8)) << 32) | (0xffffffffu << v), md = ((u64)(0xffffffffu >> (v + 8)) << 32) | (0xffffffffu >> v);
        at[i] = __builtin_amdgcn_inverse_ballot_w64(hf ? mu : md) ? at[i] : 0.f;
      }
      pf[0][0] = pack8(at, 0); pf[0][1] = pack8(at, 1);
    }
    if (hf) {
      f32x16 at = zero16();
      u32x4 kr[8];
#pragma unroll
      for (int ks = 0; ks < 8; ++ks) kr[ks] = ldsr128((ks & 1 ? k1 : k0) + (ks >> 1) * 512);
      SB();
#pragma unroll
      for (int ks = 0; ks < 8; ++ks) at = mfma32(kr[ks], qh[ks], at);
      pf[1][0] = pack8(at, 0); pf[1][1] = pack8(at, 1);
    }
    PIN(pf[0][0]); PIN(pf[0][1]); PIN(pf[1][0]); PIN(pf[1][1]);
  }
  f32x16 o[4];
  {
    const u32 v0 = tro0 + R_V + 8192 * c, v1 = tro1 + R_V + 8192 * c;
    {
      const u32 a0 = v0 + hf * 4096, a1 = v1 + hf * 4096;
#pragma unroll
      for (int s = 0; s < 2; ++s) {
        u32x4 vf[4];
#pragma unroll
        for (int i = 0; i < 4; ++i) { const int of = (i >> 1) * 32768 + (i & 1) * 512 + s * 2048; vf[i] = trfrag(a0 + of, a1 + of); }
#pragma unroll
        for (int i = 0; i < 4; ++i) o[i] = mfma32(vf[i], pf[0][s], s ? o[i] : zero16());
      }
    }
    if (hf) {
#pragma unroll
      for (int s = 0; s < 2; ++s) {
        u32x4 vf[4];
#pragma unroll
        for (int i = 0; i < 4; ++i) { const int of = (i >> 1) * 32768 + (i & 1) * 512 + s * 2048; vf[i] = trfrag(v0 + of, v1 + of); }
#pragma unroll
        for (int i = 0; i < 4; ++i) o[i] = mfma32(vf[i], pf[1][s], o[i]);
      }
    }
  }
  FENCE();
  f32x2 lo[4], hi[4];
  gather<0>(in, hd, lo, hi);
  PIN(fl);
  await<1>(fl, hd);
  load_inc<1>(in, hd);
  barrier();
  const u32 iw1 = iw0 ^ 32;
#pragma unroll
  for (int cc = 0; cc < 4; ++cc) {
    fold1(cc, 0, lo, hi, phi, sv[0]); fold1(cc, 1, lo, hi, phi, sv[0]);
    ldsw128(iw0 + R_K + cc * 16384, sv[0][cc][0]);
    fold1(cc, 2, lo, hi, phi, sv[0]); fold1(cc, 3, lo, hi, phi, sv[0]);
    ldsw128(iw1 + R_K + cc * 16384, sv[0][cc][1]);
    SB();
  }
  FENCE();
  gather<1>(in, hd, lo, hi);
  put_tag(hd, 112, seg == 7);
  barrier();
  {
    const u32 s0 = tro0 + R_K + c * 16384, s1 = tro1 + R_K + c * 16384;
    u32x4 sf[16];
#pragma unroll
    for (int i = 0; i < 16; ++i) sf[i] = trfrag(s0 + (i >> 1) * 2048 + (i & 1) * 512, s1 + (i >> 1) * 2048 + (i & 1) * 512);
    SB();
#pragma unroll
    for (int i = 0; i < 16; ++i) {
      fold1(i >> 2, i & 3, lo, hi, phi, sv[1]);
      o[i & 1] = mfma32(sf[i], qh[i >> 1], o[i & 1]);
      if (i & 1) ldsw128(((i >> 1) & 1 ? iw1 : iw0) + R_V + (i >> 2) * 16384, sv[1][i >> 2][(i >> 1) & 1]);
      SB();
    }
#pragma unroll
    for (int i = 0; i < 4; ++i) ldsw128(ow ^ (i << 5), pack8(o[i >> 1], i & 1));
  }
  barrier();
  {
    const u32 s0 = tro0 + R_V + c * 16384, s1 = tro1 + R_V + c * 16384;
    u32x4 sf[16], ob[4];
#pragma unroll
    for (int i = 0; i < 16; ++i) sf[i] = trfrag(s0 + (i >> 1) * 2048 + (i & 1) * 512, s1 + (i >> 1) * 2048 + (i & 1) * 512);
#pragma unroll
    for (int y = 0; y < 4; ++y) ob[y] = ldsr128(xw + y * 1024 + lane16);
    SB();
#pragma unroll
    for (int i = 0; i < 16; ++i) {
      o[2 + (i & 1)] = mfma32(sf[i], qh[i >> 1], o[2 + (i & 1)]);
      if (i >= 10 && i < 14) __builtin_amdgcn_raw_buffer_store_b128(ob[i - 10], ro, ooff, hf ? (i - 10) * 2048 : (13 - i) * 2048, 2);
      SB();
    }
#pragma unroll
    for (int i = 0; i < 4; ++i) ldsw128(ow ^ (i << 5), pack8(o[2 + (i >> 1)], i & 1));
#pragma unroll
    for (int y = 0; y < 4; ++y) __builtin_amdgcn_raw_buffer_store_b128(ldsr128(xw + y * 1024 + lane16), ro, ooff + 128, hf ? y * 2048 : (3 - y) * 2048, 2);
  }
  asm volatile(".Lgla_end:" ::: "memory");
}

extern "C" void kernel_launch(void* const* d_in, const int* in_sizes, int n_in, void* d_out, int out_size, void* d_ws, size_t ws_size,
                              hipStream_t stream) {
  (void)hipFuncSetAttribute((const void*)gla_kernel, hipFuncAttributeMaxDynamicSharedMemorySize, LDS_BYTES);
  gla_kernel<<<256, 512, LDS_BYTES, stream>>>(d_in[0], d_in[1], d_in[2], d_in[3], d_out, (char*)d_ws);
}
```
